```python
import torch

def _softmax(x, axis=-1):
    return torch.softmax(x, dim=axis)
def _gelu(x):
    return torch.nn.functional.gelu(x, approximate="tanh")
def _silu(x):
    return torch.nn.functional.silu(x)
def _sigmoid(x):
    return torch.sigmoid(x)
def _layernorm(x, axis=-1, eps=1e-5):
    m = x.mean(dim=axis, keepdim=True)
    v = x.var(dim=axis, keepdim=True, unbiased=False)
    return (x - m) / torch.sqrt(v + eps)
def _scatter_add(tgt, idx, src, axis=0):
    return tgt.clone().index_add_(axis, idx, src)
def _segment_sum(src, seg, n):
    out = torch.zeros((n,) + src.shape[1:], dtype=src.dtype)
    return out.index_add_(0, seg, src)
def _pad(x, pw):
    flat = tuple(v for pair in reversed(pw) for v in pair)
    return torch.nn.functional.pad(x, flat)

kernel_name = 'gated_mix__gqa_block_cached__geglu_ffn__decode'

def setup_inputs():

    _w = (lambda a: (lambda t: t.to(torch.bfloat16) if t.is_floating_point() else t)(torch.as_tensor(a)))
    def _A_setup():
        B, Sq, D = 1, 1, 448
        Hq, Hkv, Sk, Dh = 16, 8, 65536, 256
        assert Hq % Hkv == 0, (Hq, Hkv)
        return {
            "x": _w((lambda *s: torch.randn(*s, dtype=torch.bfloat16) * 0.05)(B, Sq, D)),
            "k_cache": _w((lambda *s: torch.randn(*s, dtype=torch.bfloat16) * 6.4)(B, Hkv, Sk, Dh)),
            "v_cache": _w((lambda *s: torch.randn(*s, dtype=torch.bfloat16) * 0.2)(B, Hkv, Sk, Dh)),
            "wq": _w((lambda *s: torch.randn(*s, dtype=torch.bfloat16) * 6.4)(D, Hq * Dh) * (D ** -0.5)),
            "wo": _w((lambda *s: torch.randn(*s, dtype=torch.bfloat16) * 0.2)(Hq * Dh, D) * ((Hq * Dh) ** -0.5)),
        }
    _A_in = _A_setup()

    def _B_setup():
        B, S, D, F = 1, 1, 448, 1792
        return {
            "x": _w((lambda *s: torch.randn(*s, dtype=torch.bfloat16) * 0.05)(B, S, D)),
            "w_gate": _w((lambda *s: torch.randn(*s, dtype=torch.bfloat16) * 0.05)(D, F)),
            "w_up": _w((lambda *s: torch.randn(*s, dtype=torch.bfloat16) * 0.05)(D, F)),
            "w_down": _w((lambda *s: torch.randn(*s, dtype=torch.bfloat16) * 0.05)(F, D)),
        }
    _B_in = _B_setup()

    return {
        "A_x": _A_in["x"],
        "A_k_cache": _A_in["k_cache"],
        "A_v_cache": _A_in["v_cache"],
        "A_wq": _A_in["wq"],
        "A_wo": _A_in["wo"],
        "B_x": _B_in["x"],
        "B_w_gate": _B_in["w_gate"],
        "B_w_up": _B_in["w_up"],
        "B_w_down": _B_in["w_down"],
        "gate_w": _w((lambda *s: torch.randn(*s, dtype=torch.bfloat16) * 0.05)(448) * 16.0),
    }

def reference(A_x, A_k_cache, A_v_cache, A_wq, A_wo, B_x, B_w_gate, B_w_up, B_w_down, gate_w):
    def _A_ref(x, k_cache, v_cache, wq, wo):
        B, Sq, D = x.shape
        Hq, Dh = 16, 256
        Hkv = k_cache.shape[1]
        rep = Hq // Hkv
        q = (lambda x, *d: x.permute(*d))((x @ wq).reshape(B, Sq, Hq, Dh), 0, 2, 1, 3)
        k = (lambda x, n, axis: torch.repeat_interleave(x, n, dim=axis))(k_cache, rep, axis=1)
        v = (lambda x, n, axis: torch.repeat_interleave(x, n, dim=axis))(v_cache, rep, axis=1)
        scores = torch.einsum("bhqd,bhkd->bhqk", q, k) * (Dh ** -0.5)
        p = _softmax(scores, axis=-1)
        o = torch.einsum("bhqk,bhkd->bhqd", p, v)
        return (lambda x, *d: x.permute(*d))(o, 0, 2, 1, 3).reshape(B, Sq, Hq * Dh) @ wo

    def _B_ref(x, w_gate, w_up, w_down):
        return (_gelu(x @ w_gate) * (x @ w_up)) @ w_down

    x = A_x
    g = _sigmoid((x * gate_w).sum(-1, keepdims=True))
    a = _A_ref(x, A_k_cache, A_v_cache, A_wq, A_wo)
    b = _B_ref(x, B_w_gate, B_w_up, B_w_down)
    return g * a + (1 - g) * b
```

```cpp
#include <hip/hip_runtime.h>
#include <hip/hip_bf16.h>
#include <cstdint>

typedef unsigned short bf16_t;
typedef unsigned v4u __attribute__((ext_vector_type(4)));
__device__ __forceinline__ float bf2f(unsigned short b) { return __builtin_bit_cast(float, (unsigned)b << 16); }
__device__ __forceinline__ float bflo(unsigned u) { return __builtin_bit_cast(float, u << 16); }
__device__ __forceinline__ float bfhi(unsigned u) { return __builtin_bit_cast(float, u & 0xffff0000u); }
__device__ __forceinline__ unsigned short f2bf(float f) { unsigned u = __builtin_bit_cast(unsigned, f); u += 0x7fffu + ((u >> 16) & 1u); return (unsigned short)(u >> 16); }

constexpr int D = 448, HQ = 16, HKV = 8, SK = 65536, DH = 256, F = 1792;
constexpr int NSPLIT = 32;

__global__ void k_pre(const bf16_t* x, const bf16_t* wq, const bf16_t* wg, const bf16_t* wu, float* q, float* h) {
  __shared__ float xs[D];
  for (int i = threadIdx.x; i < D; i += blockDim.x) xs[i] = bf2f(x[i]);
  __syncthreads();
  int c = blockIdx.x * blockDim.x + threadIdx.x;
  if (c < HQ * DH) {
    float a = 0.f;
    for (int r = 0; r < D; ++r) a += xs[r] * bf2f(wq[(size_t)r * (HQ * DH) + c]);
    q[c] = bf2f(f2bf(a));
  } else if (c < HQ * DH + F) {
    int j = c - HQ * DH;
    float g = 0.f, u = 0.f;
    for (int r = 0; r < D; ++r) { g += xs[r] * bf2f(wg[(size_t)r * F + j]); u += xs[r] * bf2f(wu[(size_t)r * F + j]); }
    float t = 0.7978845608028654f * (g + 0.044715f * g * g * g);
    float ge = 0.5f * g * (1.f + tanhf(t));
    h[j] = ge * u;
  }
}

__global__ void __launch_bounds__(256) k_attn(const bf16_t* K, const bf16_t* V, const float* q, float* pm, float* pl, float* pacc) {
  const int s = blockIdx.x, kvh = blockIdx.y;
  const int tid = threadIdx.x, lane = tid & 63, wave = tid >> 6, c = lane & 31, r = lane >> 5;
  float q0[8], q1[8];
  for (int k = 0; k < 8; ++k) { q0[k] = q[(kvh * 2) * DH + 8 * c + k]; q1[k] = q[(kvh * 2 + 1) * DH + 8 * c + k]; }
  const int keys = SK / NSPLIT;
  const bf16_t* Kb = K + ((size_t)kvh * SK + (size_t)s * keys) * DH;
  const bf16_t* Vb = V + ((size_t)kvh * SK + (size_t)s * keys) * DH;
  float m0 = -INFINITY, m1 = -INFINITY, l0 = 0.f, l1 = 0.f;
  float a0[8] = {0}, a1[8] = {0};
  for (int t = wave * 2 + r; t < keys; t += 8) {
    v4u kv = *(const v4u*)(Kb + (size_t)t * DH + 8 * c);
    v4u vv = *(const v4u*)(Vb + (size_t)t * DH + 8 * c);
    float kf[8] = {bflo(kv.x), bfhi(kv.x), bflo(kv.y), bfhi(kv.y), bflo(kv.z), bfhi(kv.z), bflo(kv.w), bfhi(kv.w)};
    float vf[8] = {bflo(vv.x), bfhi(vv.x), bflo(vv.y), bfhi(vv.y), bflo(vv.z), bfhi(vv.z), bflo(vv.w), bfhi(vv.w)};
    float s0 = 0.f, s1 = 0.f;
    for (int k = 0; k < 8; ++k) { s0 += kf[k] * q0[k]; s1 += kf[k] * q1[k]; }
    for (int o = 16; o >= 1; o >>= 1) { s0 += __shfl_xor(s0, o); s1 += __shfl_xor(s1, o); }
    s0 *= 0.0625f; s1 *= 0.0625f;
    float n0 = fmaxf(m0, s0), n1 = fmaxf(m1, s1);
    float al0 = __expf(m0 - n0), al1 = __expf(m1 - n1), p0 = __expf(s0 - n0), p1 = __expf(s1 - n1);
    l0 = l0 * al0 + p0; l1 = l1 * al1 + p1; m0 = n0; m1 = n1;
    for (int k = 0; k < 8; ++k) { a0[k] = a0[k] * al0 + p0 * vf[k]; a1[k] = a1[k] * al1 + p1 * vf[k]; }
  }
  __shared__ float sm[8][2], sl[8][2], sa[8][2][DH];
  const int rec = wave * 2 + r;
  if (c == 0) { sm[rec][0] = m0; sm[rec][1] = m1; sl[rec][0] = l0; sl[rec][1] = l1; }
  for (int k = 0; k < 8; ++k) { sa[rec][0][8 * c + k] = a0[k]; sa[rec][1][8 * c + k] = a1[k]; }
  __syncthreads();
  for (int h = 0; h < 2; ++h) {
    float M = -INFINITY;
    for (int i = 0; i < 8; ++i) M = fmaxf(M, sm[i][h]);
    float L = 0.f, o = 0.f;
    for (int i = 0; i < 8; ++i) { float w = __expf(sm[i][h] - M); L += w * sl[i][h]; o += w * sa[i][h][tid]; }
    size_t slot = (size_t)(kvh * 2 + h) * NSPLIT + s;
    pacc[slot * DH + tid] = o;
    if (tid == 0) { pm[slot] = M; pl[slot] = L; }
  }
}

__global__ void k_comb(const float* pm, const float* pl, const float* pacc, float* o) {
  const int h = blockIdx.x, tid = threadIdx.x;
  float M = -INFINITY;
  for (int s = 0; s < NSPLIT; ++s) M = fmaxf(M, pm[h * NSPLIT + s]);
  float L = 0.f, a = 0.f;
  for (int s = 0; s < NSPLIT; ++s) { float w = __expf(pm[h * NSPLIT + s] - M); L += w * pl[h * NSPLIT + s]; a += w * pacc[((size_t)h * NSPLIT + s) * DH + tid]; }
  o[h * DH + tid] = a / L;
}

__global__ void k_out(const bf16_t* x, const bf16_t* gw, const float* o, const float* h, const bf16_t* wo, const bf16_t* wd, __hip_bfloat16* out) {
  __shared__ float red[256];
  const int j = blockIdx.x, tid = threadIdx.x;
  float a = 0.f, b = 0.f, g = 0.f;
  for (int r = tid; r < HQ * DH; r += 256) a += o[r] * bf2f(wo[(size_t)r * D + j]);
  for (int r = tid; r < F; r += 256) b += h[r] * bf2f(wd[(size_t)r * D + j]);
  for (int r = tid; r < D; r += 256) g += bf2f(x[r]) * bf2f(gw[r]);
  float v[3] = {a, b, g};
  float res[3];
  for (int i = 0; i < 3; ++i) {
    red[tid] = v[i]; __syncthreads();
    for (int st = 128; st >= 1; st >>= 1) { if (tid < st) red[tid] += red[tid + st]; __syncthreads(); }
    res[i] = red[0]; __syncthreads();
  }
  if (tid == 0) {
    float gg = 1.f / (1.f + __expf(-res[2]));
    float r = gg * res[0] + (1.f - gg) * res[1];
    ((bf16_t*)out)[j] = f2bf(r);
  }
}

extern "C" void kernel_launch(void* const* d_in, const int* in_sizes, int n_in, void* d_out, int out_size, void* d_ws, size_t ws_size, hipStream_t stream) {
  const bf16_t* x = (const bf16_t*)d_in[0];
  const bf16_t* K = (const bf16_t*)d_in[1];
  const bf16_t* V = (const bf16_t*)d_in[2];
  const bf16_t* wq = (const bf16_t*)d_in[3];
  const bf16_t* wo = (const bf16_t*)d_in[4];
  const bf16_t* wg = (const bf16_t*)d_in[6];
  const bf16_t* wu = (const bf16_t*)d_in[7];
  const bf16_t* wd = (const bf16_t*)d_in[8];
  const bf16_t* gw = (const bf16_t*)d_in[9];
  float* ws = (float*)d_ws;
  float* q = ws; float* h = ws + 4096; float* o = ws + 8192; float* pm = ws + 12288; float* pl = ws + 13312; float* pacc = ws + 16384;
  k_pre<<<(4096 + 1792 + 63) / 64, 64, 0, stream>>>(x, wq, wg, wu, q, h);
  k_attn<<<dim3(NSPLIT, HKV), 256, 0, stream>>>(K, V, q, pm, pl, pacc);
  k_comb<<<HQ, 256, 0, stream>>>(pm, pl, pacc, o);
  k_out<<<D, 256, 0, stream>>>(x, gw, o, h, wo, wd, (__hip_bfloat16*)d_out);
}
```

```cpp
#include <hip/hip_runtime.h>
#include <hip/hip_bf16.h>
#include <stdint.h>

#define GAS __attribute__((address_space(1)))
#define LAS __attribute__((address_space(3)))
#define CAS __attribute__((address_space(4)))
typedef unsigned v4u __attribute__((ext_vector_type(4)));
typedef unsigned v2u __attribute__((ext_vector_type(2)));
typedef float f32x4 __attribute__((ext_vector_type(4)));
typedef float f32x2 __attribute__((ext_vector_type(2)));
typedef __bf16 bf16x2 __attribute__((ext_vector_type(2)));
typedef unsigned long long u64;

namespace {
constexpr int D = 448, HKV = 8, SK = 65536, DH = 256, FF = 1792, QC = 4096;
constexpr int NWG = 256, NTHR = 512, NW = 8;
constexpr int NSPLIT = 32;
constexpr int KEYS_WG = SK / NSPLIT;
constexpr int T = 4;
constexpr int ST = 8;
constexpr int NT = KEYS_WG / (NW * 2 * T);
constexpr unsigned CH_BYTES = KEYS_WG * DH * 2;
constexpr unsigned TILE_B = T * 1024, ROUND_B = NW * TILE_B;
constexpr int NRED = 28;
constexpr float C2 = 0.0625f * 1.4426950408889634f;
constexpr size_t WS_EPOCH = 0;
constexpr size_t WS_G1M = 4096;
constexpr size_t WS_G1A = WS_G1M + 8 * 2 * 32 * 2 * 8;
constexpr size_t WS_G2 = WS_G1A + (size_t)8 * 32 * 32 * 16 * 8;
constexpr size_t WS_END = WS_G2 + (size_t)NRED * 256 * 16 * 8;
constexpr size_t WS_DBG = 4u << 20;
constexpr int L_QP = 0;
constexpr int L_P = L_QP + 8 * 512 * 4;
constexpr int L_ML = L_P + 8 * 512 * 4;
constexpr int L_FP = L_ML + 128;
constexpr int L_H1 = L_FP + 17 * 8 * 4 + 32;
constexpr int L_H2 = L_H1 + 16 * 8 * 4;
constexpr int L_END = L_H2 + 16 * 8 * 4;
static_assert(L_FP % 16 == 0 && L_H1 % 16 == 0 && L_H2 % 16 == 0, "align");
constexpr unsigned kSpin = 1u << 18;

#define AUX_WQ 0

__device__ __forceinline__ float dot2(unsigned a, unsigned b, float c) {
  return __builtin_amdgcn_fdot2_f32_bf16(__builtin_bit_cast(bf16x2, a), __builtin_bit_cast(bf16x2, b), c, false);
}
__device__ __forceinline__ float as_f(unsigned u) { return __builtin_bit_cast(float, u); }
__device__ __forceinline__ unsigned as_u(float f) { return __builtin_bit_cast(unsigned, f); }
__device__ __forceinline__ float bflo(unsigned u) { return as_f(u << 16); }
__device__ __forceinline__ float bfhi(unsigned u) { return as_f(u & 0xffff0000u); }
__device__ __forceinline__ unsigned f2bf(float f) { unsigned u = as_u(f); u += 0x7fffu + ((u >> 16) & 1u); return u >> 16; }
template <int CTRL> __device__ __forceinline__ float dpp(float x) {
  return as_f(__builtin_amdgcn_mov_dpp((int)as_u(x), CTRL, 0xf, 0xf, true));
}
constexpr int XOR1 = 0xB1, XOR2 = 0x4E, XOR7 = 0x141, XOR8 = 0x128, ROR4 = 0x124;
__device__ __forceinline__ float sw16_add(float a, float b) {
  auto s = __builtin_amdgcn_permlane16_swap(as_u(a), as_u(b), false, false);
  return as_f(s[0]) + as_f(s[1]);
}
__device__ __forceinline__ float sw32_add(float a, float b) {
  auto s = __builtin_amdgcn_permlane32_swap(as_u(a), as_u(b), false, false);
  return as_f(s[0]) + as_f(s[1]);
}
__device__ __forceinline__ float sw16_max(float a) { auto s = __builtin_amdgcn_permlane16_swap(as_u(a), as_u(a), false, false); return fmaxf(as_f(s[0]), as_f(s[1])); }
__device__ __forceinline__ float sw32_max(float a) { auto s = __builtin_amdgcn_permlane32_swap(as_u(a), as_u(a), false, false); return fmaxf(as_f(s[0]), as_f(s[1])); }
__device__ __forceinline__ float tr_reduce8_32(const float (&x)[8], int lane) {
  float y[4], z[2];
#pragma unroll
  for (int v = 0; v < 4; ++v) y[v] = sw16_add(x[v], x[v + 4]);
#pragma unroll
  for (int v = 0; v < 2; ++v) { const float t1 = y[v] + dpp<XOR8>(y[v]), t2 = y[v + 2] + dpp<XOR8>(y[v + 2]); z[v] = (lane & 8) ? t2 : t1; }
  const float t1 = z[0] + dpp<XOR7>(z[0]), t2 = z[1] + dpp<XOR7>(z[1]);
  float u = (lane & 4) ? t2 : t1;
  u += dpp<XOR2>(u); u += dpp<XOR1>(u);
  return u;
}
__device__ __forceinline__ void wg_barrier() { asm volatile("s_waitcnt lgkmcnt(0)\n\ts_barrier" ::: "memory"); }
__device__ __forceinline__ u64 enc(float v, unsigned tl24, unsigned th) {
  const unsigned lo = ((as_u(v) + 0x80u) >> 8) | tl24;
  return ((u64)th << 32) | lo;
}
__device__ __forceinline__ float round24(float v) { return as_f((as_u(v) + 0x80u) & 0xffffff00u); }
#define RLX_AGENT __ATOMIC_RELAXED, __HIP_MEMORY_SCOPE_AGENT

struct Args {
  const unsigned short* x; const unsigned short* K; const unsigned short* V; const unsigned short* wq; const unsigned short* wo;
  const unsigned short* wg; const unsigned short* wu; const unsigned short* wd; const unsigned short* gw;
  unsigned short* out; unsigned char* ws;
};

#define STAMP(i) do {} while (0)

__global__ void __launch_bounds__(NTHR, 2) fused_kernel(Args a) {
  extern __shared__ __attribute__((aligned(16))) unsigned char smem[];
  LAS unsigned char* L = (LAS unsigned char*)smem;
  const int tid = threadIdx.x, lane = tid & 63, wave = __builtin_amdgcn_readfirstlane(tid >> 6);
  const int b = blockIdx.x, h = b & 7, s = b >> 3;
  const int r = lane >> 5, c = lane & 31;
  STAMP(0);
  const unsigned cbase = (unsigned)(h * NSPLIT + s) * CH_BYTES;
  const __amdgpu_buffer_rsrc_t rk = __builtin_amdgcn_make_buffer_rsrc((char*)a.K + cbase, 0, CH_BYTES, 0x00020000);
  const __amdgpu_buffer_rsrc_t rv = __builtin_amdgcn_make_buffer_rsrc((char*)a.V + cbase, 0, CH_BYTES, 0x00020000);
  const __amdgpu_buffer_rsrc_t rq = __builtin_amdgcn_make_buffer_rsrc((char*)a.wq + h * 1024, 0, D * QC * 2 - h * 1024, 0x00020000);
  const unsigned voff = lane * 16;
  const unsigned off0 = wave * TILE_B;
  const int rot = s;
#define TOFF(i_) (off0 + (unsigned)(((i_) + rot) & (NT - 1)) * ROUND_B)
  v4u bu[ST][T];
#define LDR(r_, s_, so_) do { const unsigned so__ = (so_); \
  _Pragma("unroll") for (int t_ = 0; t_ < T; ++t_) bu[s_][t_] = __builtin_amdgcn_raw_buffer_load_b128(r_, voff + t_ * 1024, so__, 2); } while (0)

  const u64 ep = __hip_atomic_load((GAS u64*)(a.ws + WS_EPOCH), RLX_AGENT);
  const CAS unsigned* xs = (const CAS unsigned*)a.x + wave * 28;
  float qa[8];
#pragma unroll
  for (int k = 0; k < 8; ++k) qa[k] = 0.f;
  const int fG = (b & 7) * 4 + (s >> 3), fi = fG * 8 + (s & 7);
  const bool fvalid = fG < 28;
  v4u fg = {0, 0, 0, 0}, fu = {0, 0, 0, 0}; unsigned xt = 0, gt = 0;
  {
    v4u wb[14];
#pragma unroll
    for (int bt = 0; bt < 4; ++bt) {
#pragma unroll
      for (int j = 0; j < 14; ++j) wb[j] = __builtin_amdgcn_raw_buffer_load_b128(rq, voff, (unsigned)(wave * 56 + bt * 14 + j) * (QC * 2), AUX_WQ);
      LDR(rk, 2 * bt, TOFF(2 * bt));
      if (bt < 3) LDR(rk, 2 * bt + 1, TOFF(2 * bt + 1));
      if (bt == 0 && wave < 7) {
        xt = a.x[tid]; gt = a.gw[tid];
        if (fvalid) { fg = *(const GAS v4u*)(a.wg + (size_t)tid * FF + fi * 8); fu = *(const GAS v4u*)(a.wu + (size_t)tid * FF + fi * 8); }
      }
      __builtin_amdgcn_sched_barrier(0);
#pragma unroll
      for (int j = 0; j < 14; ++j) {
        const unsigned xd = xs[(bt * 14 + j) >> 1];
        const unsigned xlo = (j & 1) ? (xd >> 16) : (xd & 0xffffu), xhi = (j & 1) ? (xd & 0xffff0000u) : (xd << 16);
#pragma unroll
        for (int k = 0; k < 4; ++k) { qa[2 * k] = dot2(wb[j][k], xlo, qa[2 * k]); qa[2 * k + 1] = dot2(wb[j][k], xhi, qa[2 * k + 1]); }
      }
      __builtin_amdgcn_sched_barrier(0);
    }
  }
  {
    LAS f32x4* qp = (LAS f32x4*)(L + L_QP) + wave * 128 + lane * 2;
    qp[0] = f32x4{qa[0], qa[1], qa[2], qa[3]}; qp[1] = f32x4{qa[4], qa[5], qa[6], qa[7]};
  }
  if (wave < 7) {
    const float xf = bflo(xt);
    float pr[16];
#pragma unroll
    for (int k = 0; k < 4; ++k) { pr[2 * k] = xf * bflo(fg[k]); pr[2 * k + 1] = xf * bfhi(fg[k]); pr[8 + 2 * k] = xf * bflo(fu[k]); pr[8 + 2 * k + 1] = xf * bfhi(fu[k]); }
    float x8[8];
#pragma unroll
    for (int v = 0; v < 8; ++v) x8[v] = sw32_add(pr[v], pr[v + 8]);
    const float f = tr_reduce8_32(x8, lane);
    float g = xf * bflo(gt);
    g += dpp<XOR1>(g); g += dpp<XOR2>(g); g += dpp<XOR7>(g); g += dpp<XOR8>(g);
    g = sw16_add(g, g); g = sw32_add(g, g);
    const int v = ((lane >> 2) & 7) | (r << 3);
    if ((lane & 3) == 0) ((LAS float*)(L + L_FP))[v * 8 + wave] = f;
    if (lane == 0) ((LAS float*)(L + L_FP))[16 * 8 + wave] = g;
  }
  STAMP(1);
  wg_barrier();
  v4u q0, q1;
  {
    const LAS f32x4* qp = (const LAS f32x4*)(L + L_QP) + r * 64 + c * 2;
    f32x4 s0 = qp[0], s1 = qp[1];
#pragma unroll
    for (int w = 1; w < 8; ++w) { s0 += qp[w * 128]; s1 += qp[w * 128 + 1]; }
    unsigned pk[4] = {f2bf(s0[0]) | (f2bf(s0[1]) << 16), f2bf(s0[2]) | (f2bf(s0[3]) << 16), f2bf(s1[0]) | (f2bf(s1[1]) << 16), f2bf(s1[2]) | (f2bf(s1[3]) << 16)};
#pragma unroll
    for (int k = 0; k < 4; ++k) { auto sw = __builtin_amdgcn_permlane32_swap(pk[k], pk[k], false, false); q0[k] = sw[0]; q1[k] = sw[1]; }
  }
  unsigned ep_lo = __builtin_amdgcn_readfirstlane((unsigned)ep), ep_hi = __builtin_amdgcn_readfirstlane((unsigned)(ep >> 32));
  const u64 ep1 = (((u64)ep_hi << 32) | ep_lo) + 1;
  const unsigned tl24 = ((unsigned)ep1 & 0xffu) << 24, th = (unsigned)(ep1 >> 8);
  STAMP(2);

  LAS float* P = (LAS float*)(L + L_P) + wave * 512;
  const int pidx = (lane & 3) * 16 + ((lane >> 2) & 1) * 8 + r * 4 + (((lane >> 4) & 1) * 2 + ((lane >> 3) & 1));
  float sacc = 0.f;
#define KTILE(s_) do { float x_[8]; \
    _Pragma("unroll") for (int t_ = 0; t_ < T; ++t_) { float d0 = 0.f, d1 = 0.f; \
      _Pragma("unroll") for (int k_ = 0; k_ < 4; ++k_) { d0 = dot2(bu[s_][t_][k_], q0[k_], d0); d1 = dot2(bu[s_][t_][k_], q1[k_], d1); } \
      x_[2 * t_] = d0; x_[2 * t_ + 1] = d1; } \
    const float u_ = tr_reduce8_32(x_, lane); \
    sacc = ((lane & 3) == ((s_) & 3)) ? u_ : sacc; } while (0)
#pragma unroll 1
  for (int i = 0; i < NT - ST; i += ST) {
#pragma unroll
    for (int sl = 0; sl < ST; ++sl) {
      const int sn = (sl + ST - 1) % ST;
      LDR(rk, sn, TOFF(i + sl + ST - 1));
      __builtin_amdgcn_sched_barrier(0);
      KTILE(sl);
      if ((sl & 3) == 3) P[((i + sl) >> 2) * 64 + pidx] = sacc;
      __builtin_amdgcn_sched_barrier(0);
    }
  }
#pragma unroll
  for (int sl = 0; sl < ST; ++sl) {
    const int sn = (sl + ST - 1) % ST;
    if (sl == 0) LDR(rk, sn, TOFF(NT - 1)); else LDR(rv, sn, TOFF(sl - 1));
    __builtin_amdgcn_sched_barrier(0);
    KTILE(sl);
    if ((sl & 3) == 3) P[((NT - ST + sl) >> 2) * 64 + pidx] = sacc;
    __builtin_amdgcn_sched_barrier(0);
  }
  STAMP(3);
  float mw, lw;
  {
    float sc[8];
#pragma unroll
    for (int g = 0; g < 8; ++g) sc[g] = P[g * 64 + pidx];
    float m = sc[0];
#pragma unroll
    for (int g = 1; g < 8; ++g) m = fmaxf(m, sc[g]);
    m = fmaxf(m, dpp<XOR1>(m)); m = fmaxf(m, dpp<XOR2>(m)); m = fmaxf(m, dpp<XOR8>(m)); m = sw16_max(m); m = sw32_max(m);
    m *= C2;
    float l = 0.f;
#pragma unroll
    for (int g = 0; g < 8; ++g) { const float p = __builtin_amdgcn_exp2f(fmaf(sc[g], C2, -m)); l += p; P[g * 64 + pidx] = p; }
    l += dpp<XOR1>(l); l += dpp<XOR2>(l); l += dpp<XOR8>(l); l = sw16_add(l, l); l = sw32_add(l, l);
    mw = m; lw = l;
  }
  if ((lane & ~4) == 0) { ((LAS float*)(L + L_ML))[((lane >> 2) & 1) * 8 + wave] = mw; ((LAS float*)(L + L_ML))[16 + ((lane >> 2) & 1) * 8 + wave] = lw; }
  f32x2 acc0[4], acc1[4];
#pragma unroll
  for (int k = 0; k < 4; ++k) { acc0[k] = f32x2{0.f, 0.f}; acc1[k] = f32x2{0.f, 0.f}; }
  const LAS f32x4* P4 = (const LAS f32x4*)P + r;
  unsigned wov[16], wdv[8];
#define VTILE(s_, i_) do { \
    const f32x4 p0_ = P4[(i_) * 4], p1_ = P4[(i_) * 4 + 2]; \
    _Pragma("unroll") for (int t_ = 0; t_ < T; ++t_) { \
      _Pragma("unroll") for (int k_ = 0; k_ < 4; ++k_) { const f32x2 v_ = {bflo(bu[s_][t_][k_]), bfhi(bu[s_][t_][k_])}; \
        acc0[k_] = __builtin_elementwise_fma(f32x2{p0_[t_], p0_[t_]}, v_, acc0[k_]); \
        acc1[k_] = __builtin_elementwise_fma(f32x2{p1_[t_], p1_[t_]}, v_, acc1[k_]); } } \
    asm volatile("" : "+v"(acc0[0]), "+v"(acc0[1]), "+v"(acc0[2]), "+v"(acc0[3]), "+v"(acc1[0]), "+v"(acc1[1]), "+v"(acc1[2]), "+v"(acc1[3])); } while (0)
#pragma unroll 1
  for (int i = 0; i < NT - ST; i += ST) {
#pragma unroll
    for (int sl = 0; sl < ST; ++sl) {
      const int sn = (sl + ST - 1) % ST;
      LDR(rv, sn, TOFF(i + sl + ST - 1));
      __builtin_amdgcn_sched_barrier(0);
      VTILE(sl, i + sl);
      __builtin_amdgcn_sched_barrier(0);
    }
  }
  {
    LDR(rv, ST - 1, TOFF(NT - 1));
    if (tid < 224) {
      const GAS unsigned* wop = (const GAS unsigned*)(a.wo + (size_t)(h * 512 + s * 16) * D) + tid;
#pragma unroll
      for (int j = 0; j < 16; ++j) wov[j] = wop[j * (D / 2)];
      const GAS unsigned* wdp = (const GAS unsigned*)(a.wd + (size_t)((fvalid ? fi : 0) * 8) * D) + tid;
#pragma unroll
      for (int j = 0; j < 8; ++j) wdv[j] = wdp[j * (D / 2)];
    }
    __builtin_amdgcn_sched_barrier(0);
#pragma unroll
    for (int sl = 0; sl < ST; ++sl) { VTILE(sl, NT - ST + sl); __builtin_amdgcn_sched_barrier(0); }
  }
  STAMP(4);
  {
    float o[8];
#pragma unroll
    for (int k = 0; k < 8; ++k) o[k] = sw32_add(acc0[k >> 1][k & 1], acc1[k >> 1][k & 1]);
    LAS f32x4* mg = (LAS f32x4*)(L + L_QP) + wave * 128 + lane * 2;
    mg[0] = f32x4{o[0], o[1], o[2], o[3]}; mg[1] = f32x4{o[4], o[5], o[6], o[7]};
  }
  wg_barrier();
  STAMP(5);
  const int hh = s >> 4;
  {
    const int qh = tid >> 8;
    const LAS f32x4* ml = (const LAS f32x4*)(L + L_ML);
    const f32x4 ma = ml[qh * 2], mb = ml[qh * 2 + 1], la = ml[4 + qh * 2], lb = ml[4 + qh * 2 + 1];
    float M = fmaxf(fmaxf(fmaxf(ma[0], ma[1]), fmaxf(ma[2], ma[3])), fmaxf(fmaxf(mb[0], mb[1]), fmaxf(mb[2], mb[3])));
    M = round24(M);
    const LAS float* mg = (const LAS float*)(L + L_QP) + tid;
    float o = 0.f, Ls = 0.f;
#pragma unroll
    for (int w = 0; w < 8; ++w) {
      const float wt = __builtin_amdgcn_exp2f((w < 4 ? ma[w & 3] : mb[w & 3]) - M);
      o = fmaf(wt, mg[w * 512], o); Ls = fmaf(wt, (w < 4 ? la[w & 3] : lb[w & 3]), Ls);
    }
    const int d = tid & 255, cons = qh * 16 + (d >> 4);
    GAS u64* g1 = (GAS u64*)(a.ws + WS_G1A) + (((size_t)h * 32 + cons) * 32 + s) * 16 + (d & 15);
    __hip_atomic_store(g1, enc(o, tl24, th), RLX_AGENT);
    if (d < 2) { GAS u64* gm = (GAS u64*)(a.ws + WS_G1M) + (((size_t)h * 2 + qh) * 32 + s) * 2 + d; __hip_atomic_store(gm, enc(d == 0 ? M : Ls, tl24, th), RLX_AGENT); }
  }
  STAMP(6);
  float f0 = 0.f, f1 = 0.f, gate = 0.f;
  if (wave < 4) {
    const LAS f32x4* fp = (const LAS f32x4*)(L + L_FP);
    const int v = lane & 15;
    const f32x4 pa = fp[v * 2], pb = fp[v * 2 + 1], ga = fp[32], gb = fp[33];
    const float sv = ((pa[0] + pa[1]) + (pa[2] + pa[3])) + ((pb[0] + pb[1]) + pb[2]);
    const float gs = ((ga[0] + ga[1]) + (ga[2] + ga[3])) + ((gb[0] + gb[1]) + gb[2]);
    gate = 1.f / (1.f + __expf(-gs));
    const float up = dpp<XOR8>(sv);
    const float y = 0.7978845608028654f * (sv + 0.044715f * sv * sv * sv);
    const float th_ = 1.f - 2.f / (__expf(2.f * y) + 1.f);
    float hv = 0.5f * sv * (1.f + th_) * up;
    hv = fvalid ? hv : 0.f;
#pragma unroll
    for (int j = 0; j < 8; ++j) { const float hj = as_f(__builtin_amdgcn_readlane(as_u(hv), j)); f0 = fmaf(hj, bflo(wdv[j]), f0); f1 = fmaf(hj, bfhi(wdv[j]), f1); }
    f0 *= (1.f - gate); f1 *= (1.f - gate);
  }
  {
    const int ps = wave * 4 + (lane >> 4);
    GAS u64* ga = (GAS u64*)(a.ws + WS_G1A) + (((size_t)h * 32 + s) * 32 + ps) * 16 + (lane & 15);
    GAS u64* gm = (GAS u64*)(a.ws + WS_G1M) + ((size_t)h * 2 + hh) * 64;
    u64 va, vm, vo;
    for (unsigned spin = 0;; ++spin) {
      va = __hip_atomic_load(ga, RLX_AGENT);
      vm = __hip_atomic_load(gm + c * 2 + r, RLX_AGENT);
      vo = __hip_atomic_load(gm + ps * 2, RLX_AGENT);
      const bool ok = ((unsigned)(va >> 32) == th) & ((unsigned)(vm >> 32) == th) & ((unsigned)(vo >> 32) == th) &
                      ((((unsigned)va ^ tl24) | ((unsigned)vm ^ tl24) | ((unsigned)vo ^ tl24)) < 0x1000000u);
      if (__all(ok) || spin > kSpin) break;
      __builtin_amdgcn_s_sleep(1);
    }
    const float av = as_f((unsigned)va << 8), mv = as_f((unsigned)vm << 8), mo = as_f((unsigned)vo << 8);
    auto sw = __builtin_amdgcn_permlane32_swap(as_u(mv), as_u(mv), false, false);
    const float Mp = as_f(sw[0]), Lp = as_f(sw[1]);
    float Mx = Mp;
    Mx = fmaxf(Mx, dpp<XOR1>(Mx)); Mx = fmaxf(Mx, dpp<XOR2>(Mx)); Mx = fmaxf(Mx, dpp<XOR7>(Mx)); Mx = fmaxf(Mx, dpp<XOR8>(Mx)); Mx = sw16_max(Mx);
    float Lt = __builtin_amdgcn_exp2f(Mp - Mx) * Lp;
    Lt += dpp<XOR1>(Lt); Lt += dpp<XOR2>(Lt); Lt += dpp<XOR7>(Lt); Lt += dpp<XOR8>(Lt); Lt = sw16_add(Lt, Lt);
    float t = av * __builtin_amdgcn_exp2f(mo - Mx) * (1.f / Lt);
    t = sw16_add(t, t); t = sw32_add(t, t);
    if (lane < 16) ((LAS float*)(L + L_H1))[lane * 8 + wave] = t;
  }
  STAMP(7);
  wg_barrier();
  if (wave < 4) {
    const LAS f32x4* hp = (const LAS f32x4*)(L + L_H1) + (lane & 15) * 2;
    const f32x4 pa = hp[0], pb = hp[1];
    const float ov = ((pa[0] + pa[1]) + (pa[2] + pa[3])) + ((pb[0] + pb[1]) + (pb[2] + pb[3]));
    float r0 = 0.f, r1 = 0.f;
#pragma unroll
    for (int j = 0; j < 16; ++j) { const float oj = as_f(__builtin_amdgcn_readlane(as_u(ov), j)); r0 = fmaf(oj, bflo(wov[j]), r0); r1 = fmaf(oj, bfhi(wov[j]), r1); }
    r0 = fmaf(gate, r0, f0); r1 = fmaf(gate, r1, f1);
    if (tid < 224) {
      const u64 e0 = enc(r0, tl24, th), e1 = enc(r1, tl24, th);
      const __amdgpu_buffer_rsrc_t rg = __builtin_amdgcn_make_buffer_rsrc((char*)a.ws + WS_G2, 0, NRED * 256 * 16 * 8, 0x00020000);
      const v4u pv = {(unsigned)e0, (unsigned)(e0 >> 32), (unsigned)e1, (unsigned)(e1 >> 32)};
      __builtin_amdgcn_raw_buffer_store_b128(pv, rg, (unsigned)((((tid >> 3) * 256 + b) * 16 + (tid & 7) * 2) * 8), 0, 16);
    }
  }
  STAMP(8);
  if (b < NRED) {
    const __amdgpu_buffer_rsrc_t rg = __builtin_amdgcn_make_buffer_rsrc((char*)a.ws + WS_G2, 0, NRED * 256 * 16 * 8, 0x00020000);
    const int p = wave * 32 + c;
    const unsigned off = (unsigned)(((b * 256 + p) * 16 + r * 8) * 8);
    v4u g[4];
    for (unsigned spin = 0;; ++spin) {
#pragma unroll
      for (int k = 0; k < 4; ++k) g[k] = __builtin_amdgcn_raw_buffer_load_b128(rg, off + k * 16, 0, 16);
      bool ok = true; unsigned x = 0;
#pragma unroll
      for (int k = 0; k < 4; ++k) { ok &= (g[k][1] == th) & (g[k][3] == th); x |= (g[k][0] ^ tl24) | (g[k][2] ^ tl24); }
      ok &= x < 0x1000000u;
      if (__all(ok) || spin > kSpin) break;
      __builtin_amdgcn_s_sleep(1);
    }
    float x8[8];
#pragma unroll
    for (int k = 0; k < 4; ++k) { x8[2 * k] = as_f(g[k][0] << 8); x8[2 * k + 1] = as_f(g[k][2] << 8); }
    const float u = tr_reduce8_32(x8, lane);
    if ((lane & 3) == 0) ((LAS float*)(L + L_H2))[(r * 8 + ((lane >> 2) & 7)) * 8 + wave] = u;
    wg_barrier();
    if (tid < 16) {
      const LAS f32x4* hp = (const LAS f32x4*)(L + L_H2) + tid * 2;
      const f32x4 pa = hp[0], pb = hp[1];
      const float ov = ((pa[0] + pa[1]) + (pa[2] + pa[3])) + ((pb[0] + pb[1]) + (pb[2] + pb[3]));
      a.out[b * 16 + tid] = (unsigned short)f2bf(ov);
    }
    if (b == 0 && tid == 0) __hip_atomic_store((GAS u64*)(a.ws + WS_EPOCH), ep1, RLX_AGENT);
  }
  STAMP(9);
}
}

extern "C" void kernel_launch(void* const* d_in, const int* in_sizes, int n_in, void* d_out, int out_size, void* d_ws, size_t ws_size, hipStream_t stream) {
  Args a;
  a.x = (const unsigned short*)d_in[0]; a.K = (const unsigned short*)d_in[1]; a.V = (const unsigned short*)d_in[2];
  a.wq = (const unsigned short*)d_in[3]; a.wo = (const unsigned short*)d_in[4];
  a.wg = (const unsigned short*)d_in[6]; a.wu = (const unsigned short*)d_in[7]; a.wd = (const unsigned short*)d_in[8]; a.gw = (const unsigned short*)d_in[9];
  a.out = (unsigned short*)d_out; a.ws = (unsigned char*)d_ws;
  hipLaunchKernelGGL(fused_kernel, dim3(NWG), dim3(NTHR), L_END, stream, a);
}
```

```cpp
#define QFIRST 1
#define LATE2 1
#define EARLYK 0
#define PIPE 0
#define DELTA 112
#define NSLOW 3
#define SLOW 30
#define NOTOUCH 1
#define KSTART 130
#define PDELAY 10
#define PDELAY2 12
#include <hip/hip_runtime.h>
#include <stdint.h>

#define GAS __attribute__((address_space(1)))
#define LAS __attribute__((address_space(3)))
#define CAS __attribute__((address_space(4)))
typedef unsigned v4u __attribute__((ext_vector_type(4)));
typedef unsigned v2u __attribute__((ext_vector_type(2)));
typedef unsigned v8u __attribute__((ext_vector_type(8)));
typedef float f32x4 __attribute__((ext_vector_type(4)));
typedef float f32x2 __attribute__((ext_vector_type(2)));
typedef __bf16 bf16x2 __attribute__((ext_vector_type(2)));
typedef unsigned long long u64;

#define LAG 3
#define B0 1

namespace {
constexpr int D = 448, SK = 65536, DH = 256, FF = 1792, QC = 4096;
constexpr int NWG = 256, NTHR = 512, NW = 8;
constexpr int NSPLIT = 32;
constexpr int KEYS_WG = SK / NSPLIT;
constexpr int T = 4;
constexpr int ST = 8;
constexpr int NT = KEYS_WG / (NW * 2 * T);
#define PREN 7
#define TGAP 60
#define QSLEEP 8
#define WAUX 2
#define KAUX 2
#define VAUX 2
#define SWZ 0
#define PSLEEP 2
#define HOFF 0
#define HROT 0
#define WDELAY 0
#define SLIPKEEP 0
#define SLIPDEC 0
constexpr int PRE = PREN;
constexpr int LG = LAG;
constexpr unsigned CH_BYTES = KEYS_WG * DH * 2;
constexpr unsigned TILE_B = T * 1024, ROUND_B = NW * TILE_B;
#define NREDN 28
constexpr int NRED = NREDN;
constexpr int RC = D / NRED;
constexpr int RL = RC / 4;
constexpr float C2 = 0.0625f * 1.4426950408889634f;
constexpr size_t WS_EPOCH = 0;
constexpr size_t WS_Q = 4096;
constexpr size_t WS_F = WS_Q + 8 * 8 * 4 * 64 * 8;
constexpr size_t WS_G1M = WS_F + 224 * 1024;
constexpr size_t WS_G1A = WS_G1M + 8 * 1024;
constexpr size_t WS_G2 = WS_G1A + (size_t)8 * 32 * 32 * 16 * 8;
constexpr size_t WS_END = WS_G2 + (size_t)256 * D * 8;
constexpr size_t WS_DBG = 4u << 20;
constexpr int L_MG = 0;
constexpr int L_P = L_MG + 8 * 512 * 4;
constexpr int L_ML = L_P + 8 * 512 * 4;
constexpr int L_FP = L_ML + 128;
constexpr int L_FF = L_FP + 64 * 8 * 4;
constexpr int L_HV = L_FF + 128 * 8 * 4;
constexpr int L_GM = L_HV + 64;
constexpr int L_Q = L_GM + 32;
constexpr int L_H1 = L_Q + 1024;
constexpr int L_H2 = L_H1 + 16 * 8 * 4;
constexpr int L_W = L_H2 + 16 * 8 * 4;
constexpr int L_END = L_W + 21504;
static_assert(L_FP % 16 == 0 && L_H1 % 16 == 0 && L_H2 % 16 == 0 && L_HV % 16 == 0 && L_Q % 16 == 0, "align");
constexpr unsigned kSpin = 1u << 18;

__device__ __forceinline__ float dot2(unsigned a, unsigned b, float c) {
  return __builtin_amdgcn_fdot2_f32_bf16(__builtin_bit_cast(bf16x2, a), __builtin_bit_cast(bf16x2, b), c, false);
}
__device__ __forceinline__ float as_f(unsigned u) { return __builtin_bit_cast(float, u); }
__device__ __forceinline__ unsigned as_u(float f) { return __builtin_bit_cast(unsigned, f); }
__device__ __forceinline__ float bflo(unsigned u) { return as_f(u << 16); }
__device__ __forceinline__ float bfhi(unsigned u) { return as_f(u & 0xffff0000u); }
__device__ __forceinline__ unsigned f2bf(float f) { unsigned u = as_u(f); u += 0x7fffu + ((u >> 16) & 1u); return u >> 16; }
template <int CTRL> __device__ __forceinline__ float dpp(float x) {
  return as_f(__builtin_amdgcn_mov_dpp((int)as_u(x), CTRL, 0xf, 0xf, true));
}
constexpr int XOR1 = 0xB1, XOR2 = 0x4E, XOR7 = 0x141, XOR8 = 0x128, ROR4 = 0x124;
__device__ __forceinline__ float sw16_add(float a, float b) {
  auto s = __builtin_amdgcn_permlane16_swap(as_u(a), as_u(b), false, false);
  return as_f(s[0]) + as_f(s[1]);
}
__device__ __forceinline__ float sw32_add(float a, float b) {
  auto s = __builtin_amdgcn_permlane32_swap(as_u(a), as_u(b), false, false);
  return as_f(s[0]) + as_f(s[1]);
}
__device__ __forceinline__ float sw16_max(float a) { auto s = __builtin_amdgcn_permlane16_swap(as_u(a), as_u(a), false, false); return fmaxf(as_f(s[0]), as_f(s[1])); }
__device__ __forceinline__ float sw32_max(float a) { auto s = __builtin_amdgcn_permlane32_swap(as_u(a), as_u(a), false, false); return fmaxf(as_f(s[0]), as_f(s[1])); }
__device__ __forceinline__ float tr_reduce8_32(const float (&x)[8], int lane) {
  float y[4], z[2];
#pragma unroll
  for (int v = 0; v < 4; ++v) y[v] = sw16_add(x[v], x[v + 4]);
#pragma unroll
  for (int v = 0; v < 2; ++v) { const float t1 = y[v] + dpp<XOR8>(y[v]), t2 = y[v + 2] + dpp<XOR8>(y[v + 2]); z[v] = (lane & 8) ? t2 : t1; }
  const float t1 = z[0] + dpp<XOR7>(z[0]), t2 = z[1] + dpp<XOR7>(z[1]);
  float u = (lane & 4) ? t2 : t1;
  u += dpp<XOR2>(u); u += dpp<XOR1>(u);
  return u;
}
__device__ __forceinline__ void wg_barrier() { asm volatile("s_waitcnt lgkmcnt(0)\n\ts_barrier" ::: "memory"); }
__device__ __forceinline__ u64 enc(float v, unsigned tl24, unsigned th) {
  const unsigned lo = ((as_u(v) + 0x80u) >> 8) | tl24;
  return ((u64)th << 32) | lo;
}
__device__ __forceinline__ float round24(float v) { return as_f((as_u(v) + 0x80u) & 0xffffff00u); }
#define RLX_AGENT __ATOMIC_RELAXED, __HIP_MEMORY_SCOPE_AGENT

struct Args {
  const unsigned short* wq; const unsigned short* x; unsigned char* ws; const unsigned short* K;
  const unsigned short* wg; const unsigned short* wu; const unsigned short* wo; const unsigned short* wd;
  const unsigned short* V; const unsigned short* gw; unsigned short* out;
};

#define STAMP(i) do {} while (0)
#define L_TOT L_END

__global__ void __launch_bounds__(NTHR, 2) fused_kernel(Args a) {
  extern __shared__ __attribute__((aligned(16))) unsigned char smem[];
  LAS unsigned char* L = (LAS unsigned char*)smem;
  const int tid = threadIdx.x, lane = tid & 63, wave = __builtin_amdgcn_readfirstlane(tid >> 6);
  const int b = blockIdx.x, h = (b + HOFF) & 7, s = b >> 3;
  const int r = lane >> 5, c = lane & 31;
  unsigned tgt = (unsigned)__builtin_amdgcn_s_memrealtime() + KSTART + (unsigned)(DELTA * (wave == 0 ? 8 : wave)) / 8u;
  STAMP(0);
#define CODE_PREFETCH() do {} while (0)
  const unsigned cbase = (unsigned)(h * NSPLIT + s) * CH_BYTES;
  const __amdgpu_buffer_rsrc_t rk = __builtin_amdgcn_make_buffer_rsrc((char*)a.K + cbase, 0, CH_BYTES, 0x00020000);
  const __amdgpu_buffer_rsrc_t rv = __builtin_amdgcn_make_buffer_rsrc((char*)a.V + cbase, 0, CH_BYTES, 0x00020000);
  const unsigned voff = lane * 16;
  const unsigned off0 = wave * TILE_B;
  const int rot = s + HROT * h;
#define TOFF(i_) (off0 + (unsigned)(((i_) + rot) & (NT - 1)) * ROUND_B)
  v4u bu[ST][T];
#define PACE() do { unsigned now_; while ((int)((now_ = (unsigned)__builtin_amdgcn_s_memrealtime()) - tgt) < 0) __builtin_amdgcn_s_sleep(PSLEEP); PRIO(now_); tgt += DELTA; } while (0)
#define nxt tgt
#define PRIO(now_) do {} while (0)
#define GAP(x_) do { tgt += (x_); } while (0)
#define AUX_rk KAUX
#define AUX_rv VAUX
#define LDR(r_, sl_, i_) do { const unsigned so__ = TOFF(i_); PACE(); \
  _Pragma("unroll") for (int t_ = 0; t_ < T; ++t_) bu[sl_][t_] = __builtin_amdgcn_raw_buffer_load_b128(r_, voff + t_ * 1024, so__, AUX_##r_); \
  __builtin_amdgcn_sched_barrier(0); LSTAMP(i_); } while (0)
#define LSTAMP(i_) do {} while (0)

  const bool fvalid = b < 224, w7 = wave == 7;
  const int wv = w7 ? 0 : wave;
  const int rq0 = (s >> 3) * 112 + wv * 16, rf0 = (b & 7) * 56 + wv * 8;
  const __amdgpu_buffer_rsrc_t rA = __builtin_amdgcn_make_buffer_rsrc((char*)a.wq + ((size_t)rq0 * QC + h * 512 + (s & 7) * 64) * 2, 0, w7 ? 0 : 16 * QC * 2 - 4096, 0x00020000);
  const unsigned l8 = lane >> 3, p16 = (lane & 7) * 16;
  const v4u w0 = __builtin_amdgcn_raw_buffer_load_b128(rA, l8 * (QC * 2) + p16, 0, WAUX);
  const v4u w1 = __builtin_amdgcn_raw_buffer_load_b128(rA, (l8 + 8) * (QC * 2) + p16, 0, WAUX);
  __builtin_amdgcn_sched_barrier(0);
  const v8u xs = *(const CAS v8u*)(a.x + rq0);
  const v4u xfs = *(const CAS v4u*)(a.x + rf0);
  const u64 ep1 = *(const CAS u64*)(a.ws + WS_EPOCH) + 1;
  const size_t fo = fvalid ? ((size_t)rf0 * FF + (b >> 3) * 64) * 2 : 0;
  const char* wob = (const char*)a.wo + (size_t)(h * 512 + s * 16) * (D * 2);
  const char* wdb = (const char*)a.wd + (size_t)(fvalid ? b : 0) * (8 * D * 2);
  __builtin_amdgcn_sched_barrier(0);
  v4u fg, fu, st[3];
#define LOAD2P(k_) do { \
    if ((k_) == 0) { const __amdgpu_buffer_rsrc_t rFg = __builtin_amdgcn_make_buffer_rsrc((char*)a.wg + fo, 0, (fvalid && !w7) ? 8 * FF * 2 - 1024 : 0, 0x00020000); \
      fg = __builtin_amdgcn_raw_buffer_load_b128(rFg, l8 * (FF * 2) + p16, 0, WAUX); } \
    else if ((k_) == 1) { const __amdgpu_buffer_rsrc_t rFu = __builtin_amdgcn_make_buffer_rsrc((char*)a.wu + fo, 0, (fvalid && !w7) ? 8 * FF * 2 - 1024 : 0, 0x00020000); \
      fu = __builtin_amdgcn_raw_buffer_load_b128(rFu, l8 * (FF * 2) + p16, 0, WAUX); } \
    else { const int g = wave * 3 + ((k_) - 2); \
      const char* p = g < 14 ? wob + g * 1024 : g < 21 ? wdb + (g - 14) * 1024 : g == 21 ? (const char*)a.x : (const char*)a.gw; \
      const __amdgpu_buffer_rsrc_t rS = __builtin_amdgcn_make_buffer_rsrc((char*)p, 0, g < 21 ? 1024 : g < 23 ? D * 2 : 0, 0x00020000); \
      st[(k_) - 2 < 0 ? 0 : (k_) - 2] = __builtin_amdgcn_raw_buffer_load_b128(rS, voff, 0, WAUX); } \
    __builtin_amdgcn_sched_barrier(0); } while (0)
#define LOAD2() do { LOAD2P(0); LOAD2P(1); LOAD2P(2); LOAD2P(3); LOAD2P(4); } while (0)
  if (tid == 0) ((LAS u64*)(L + L_HV))[6] = 0, ((LAS u64*)(L + L_HV))[7] = 0, ((LAS u64*)(L + L_GM))[2] = 0, ((LAS u64*)(L + L_GM))[3] = 0;
  wg_barrier();
  const unsigned tl24 = ((unsigned)ep1 & 0xffu) << 24, th = (unsigned)(ep1 >> 8);
  if (!w7) {
    const unsigned xda = (lane & 32) ? ((lane & 16) ? xs[3] : xs[2]) : ((lane & 16) ? xs[1] : xs[0]);
    const unsigned xdb = (lane & 32) ? ((lane & 16) ? xs[7] : xs[6]) : ((lane & 16) ? xs[5] : xs[4]);
    const float xa = (lane & 8) ? bfhi(xda) : bflo(xda), xb = (lane & 8) ? bfhi(xdb) : bflo(xdb);
    float pq[8];
#pragma unroll
    for (int k = 0; k < 4; ++k) { pq[2 * k] = fmaf(xa, bflo(w0[k]), xb * bflo(w1[k])); pq[2 * k + 1] = fmaf(xa, bfhi(w0[k]), xb * bfhi(w1[k])); }
    float y4[4], y2[2];
#pragma unroll
    for (int v = 0; v < 4; ++v) y4[v] = sw32_add(pq[v], pq[v + 4]);
#pragma unroll
    for (int v = 0; v < 2; ++v) y2[v] = sw16_add(y4[v], y4[v + 2]);
    const float t1 = y2[0] + dpp<XOR8>(y2[0]), t2 = y2[1] + dpp<XOR8>(y2[1]);
    const float uq = (lane & 8) ? t2 : t1;
    ((LAS float*)(L + L_FP))[((lane & 7) * 8 + (lane >> 3)) * 8 + wave] = uq;
    asm volatile("s_waitcnt lgkmcnt(0)" ::: "memory");
    unsigned arr = 0;
    if (lane == 0) arr = __hip_atomic_fetch_add((LAS unsigned*)(L + L_HV) + 14, 1u, __ATOMIC_RELAXED, __HIP_MEMORY_SCOPE_WORKGROUP);
    arr = __builtin_amdgcn_readfirstlane(arr);
    STAMP(1);
    if (arr == 6) {
      asm volatile("" ::: "memory");
      const LAS f32x4* fp = (const LAS f32x4*)(L + L_FP) + lane * 2;
      const f32x4 pa = fp[0], pb = fp[1];
      const float sv = ((pa[0] + pa[1]) + (pa[2] + pa[3])) + ((pb[0] + pb[1]) + pb[2]);
      GAS u64* gq = (GAS u64*)(a.ws + WS_Q) + ((h * 8 + (s & 7)) * 4 + (s >> 3)) * 64 + lane;
      __hip_atomic_store(gq, enc(sv, tl24, th), RLX_AGENT);
      STAMP(10);
    }
  }
  LOAD2();
  const __amdgpu_buffer_rsrc_t r1 = __builtin_amdgcn_make_buffer_rsrc((char*)a.ws + WS_G1A + (size_t)h * (32 * 32 * 16 * 8), 0, wave < 4 ? 32 * 32 * 16 * 8 : 0, 0x00020000);
  const __amdgpu_buffer_rsrc_t r2 = __builtin_amdgcn_make_buffer_rsrc((char*)a.ws + WS_G2, 0, 256 * D * 8, 0x00020000);
  const __amdgpu_buffer_rsrc_t r2r = __builtin_amdgcn_make_buffer_rsrc((char*)a.ws + WS_G2 + (size_t)(b < NRED ? b : 0) * (256 * RC * 8), 0, b < NRED ? 256 * RC * 8 : 0, 0x00020000);
#define TAGOK(g_) ((g_[1] == th) & (g_[3] == th) & (((g_[0] ^ tl24) | (g_[2] ^ tl24)) < 0x1000000u))
#define LD1() __builtin_amdgcn_raw_buffer_load_b128(r1, (unsigned)(s * 4096 + wave * 1024) + voff, 0, 16)
#define LD2(x_) do { asm volatile("" ::: "memory"); _Pragma("unroll") for (int k = 0; k < RL; ++k) x_[k] = __builtin_amdgcn_raw_buffer_load_b128(r2r, (unsigned)(wave * (32 * RC * 8) + k * 1024) + voff, 0, 16); } while (0)
  STAMP(13);
  {
    const __amdgpu_buffer_rsrc_t rg = __builtin_amdgcn_make_buffer_rsrc((char*)a.ws + WS_Q + (h * 8 + wave) * 2048, 0, 2048, 0x00020000);
    bool hq = false;
    unsigned spin = 0;
#define SWEEP() do { \
      asm volatile("" ::: "memory"); \
      const v4u g0 = __builtin_amdgcn_raw_buffer_load_b128(rg, voff, 0, 16), g1 = __builtin_amdgcn_raw_buffer_load_b128(rg, voff + 1024, 0, 16); \
      const bool ok = (g0[1] == th) & (g0[3] == th) & (g1[1] == th) & (g1[3] == th) & \
                      (((g0[0] ^ tl24) | (g0[2] ^ tl24) | (g1[0] ^ tl24) | (g1[2] ^ tl24)) < 0x1000000u); \
      hq = __all(ok) || ++spin > kSpin; \
      if (hq) { \
        float qa = as_f(g0[0] << 8) + as_f(g1[0] << 8), qb = as_f(g0[2] << 8) + as_f(g1[2] << 8); \
        qa = sw32_add(qa, qa); qb = sw32_add(qb, qb); \
        if (lane < 32) ((LAS unsigned*)(L + L_Q))[wave * 32 + lane] = f2bf(qa) | (f2bf(qb) << 16); \
        asm volatile("s_waitcnt lgkmcnt(0)" ::: "memory"); \
        if (lane == 0) __hip_atomic_fetch_add((LAS unsigned*)(L + L_HV) + 12, 1u, __ATOMIC_RELAXED, __HIP_MEMORY_SCOPE_WORKGROUP); \
        STAMP(11); } } while (0)
#pragma unroll
    for (int j = EARLYK; j < PRE; ++j) {
      if (!hq && j >= QFIRST) { do { SWEEP(); } while (!hq && (int)((unsigned)__builtin_amdgcn_s_memrealtime() - nxt) < 0); }
      LDR(rk, j, j);
      if ((j & 1) == 0) STAMP(16 + j / 2);
      if (j <= NSLOW && NSLOW) GAP(SLOW);
    }
    while (!hq) SWEEP();
  }
  {
    if (!w7) {
      const unsigned xdf = (lane & 32) ? ((lane & 16) ? xfs[3] : xfs[2]) : ((lane & 16) ? xfs[1] : xfs[0]);
      const float xc = (lane & 8) ? bfhi(xdf) : bflo(xdf);
      float pf[16];
#pragma unroll
      for (int k = 0; k < 4; ++k) { pf[2 * k] = xc * bflo(fg[k]); pf[2 * k + 1] = xc * bfhi(fg[k]); pf[8 + 2 * k] = xc * bflo(fu[k]); pf[8 + 2 * k + 1] = xc * bfhi(fu[k]); }
      float z8[8], z4[4], z2[2];
#pragma unroll
      for (int v = 0; v < 8; ++v) z8[v] = sw32_add(pf[v], pf[v + 8]);
#pragma unroll
      for (int v = 0; v < 4; ++v) z4[v] = sw16_add(z8[v], z8[v + 4]);
#pragma unroll
      for (int v = 0; v < 2; ++v) { const float a1 = z4[v] + dpp<XOR8>(z4[v]), a2 = z4[v + 2] + dpp<XOR8>(z4[v + 2]); z2[v] = (lane & 8) ? a2 : a1; }
      const int ef = ((lane >> 4) & 1) * 4 + ((lane >> 3) & 1) * 2;
      LAS float* ff = (LAS float*)(L + L_FF) + (((lane & 7) * 2 + r) * 8 + ef) * 8 + wave;
      ff[0] = z2[0]; ff[8] = z2[1];
#pragma unroll
      for (int k = 0; k < 3; ++k) *((LAS v4u*)(L + L_W + (wave * 3 + k) * 1024) + lane) = st[k];
    } else {
      float g = 0.f;
#pragma unroll
      for (int k = 0; k < 4; ++k) g = dot2(st[0][k], st[1][k], g);
      g += dpp<XOR1>(g); g += dpp<XOR2>(g); g += dpp<XOR7>(g); g += dpp<XOR8>(g);
      g = sw16_add(g, g); g = sw32_add(g, g);
      if (lane == 0) ((LAS float*)(L + L_HV))[8] = 1.f / (1.f + __expf(-g));
    }
    asm volatile("s_waitcnt lgkmcnt(0)" ::: "memory");
    unsigned arr = 0;
    if (lane == 0 && !w7) arr = __hip_atomic_fetch_add((LAS unsigned*)(L + L_HV) + 13, 1u, __ATOMIC_RELAXED, __HIP_MEMORY_SCOPE_WORKGROUP);
    arr = __builtin_amdgcn_readfirstlane(arr);
    if (arr == 6 && fvalid) {
      asm volatile("" ::: "memory");
      const LAS f32x4* fp = (const LAS f32x4*)(L + L_FF) + lane * 4;
      const f32x4 pa = fp[0], pb = fp[1], pc = fp[2], pd = fp[3];
      const float s0 = ((pa[0] + pa[1]) + (pa[2] + pa[3])) + ((pb[0] + pb[1]) + pb[2]);
      const float s1 = ((pc[0] + pc[1]) + (pc[2] + pc[3])) + ((pd[0] + pd[1]) + pd[2]);
      const u64 e0 = enc(s0, tl24, th), e1 = enc(s1, tl24, th);
      const v4u pv = {(unsigned)e0, (unsigned)(e0 >> 32), (unsigned)e1, (unsigned)(e1 >> 32)};
      const __amdgpu_buffer_rsrc_t rf = __builtin_amdgcn_make_buffer_rsrc((char*)a.ws + WS_F, 0, 224 * 1024, 0x00020000);
      __builtin_amdgcn_raw_buffer_store_b128(pv, rf, (unsigned)((((b >> 3) * 8 + (lane >> 3)) * 8 + (b & 7)) * 128 + (lane & 7) * 16), 0, 16);
    }
    volatile LAS unsigned* qcnt = (volatile LAS unsigned*)(L + L_HV) + 12;
    while (*qcnt < 8u) __builtin_amdgcn_s_sleep(1);
    asm volatile("" ::: "memory");
  }
  const v4u q0 = *((const LAS v4u*)(L + L_Q) + c), q1 = *((const LAS v4u*)(L + L_Q) + 32 + c);
  STAMP(12);

  LAS float* P = (LAS float*)(L + L_P) + wave * 512;
  const int pidx = (lane & 3) * 16 + ((lane >> 2) & 1) * 8 + r * 4 + (((lane >> 4) & 1) * 2 + ((lane >> 3) & 1));
  float sacc = 0.f;
#define KTILE(m8_, i_) do { float x_[8]; \
    _Pragma("unroll") for (int t_ = 0; t_ < T; ++t_) { float d0 = 0.f, d1 = 0.f; \
      _Pragma("unroll") for (int k_ = 0; k_ < 4; ++k_) { d0 = dot2(bu[m8_][t_][k_], q0[k_], d0); d1 = dot2(bu[m8_][t_][k_], q1[k_], d1); } \
      x_[2 * t_] = d0; x_[2 * t_ + 1] = d1; } \
    const float u_ = tr_reduce8_32(x_, lane); \
    sacc = ((lane & 3) == ((m8_) & 3)) ? u_ : sacc; \
    if (((m8_) & 3) == 3) P[((i_) >> 2) * 64 + pidx] = sacc; \
    __builtin_amdgcn_sched_barrier(0); } while (0)
#pragma unroll
  for (int i = 0; i < PRE - LG; ++i) KTILE(i, i);
#pragma unroll
  for (int n = PRE; n < ST; ++n) { LDR(rk, n, n); KTILE((n - LG) % ST, n - LG); }
#pragma unroll 1
  for (int i = ST; i < NT; i += ST) {
#pragma unroll
    for (int u = 0; u < ST; ++u) { LDR(rk, u, i + u); if (u == 0) STAMP(19 + i / 8); KTILE((u + ST - LG) % ST, i + u - LG); }
  }
#pragma unroll
  for (int n = 0; n < LG; ++n) { LDR(rv, n % ST, n); if (n == 0) STAMP(23); KTILE((NT - LG + n) % ST, NT - LG + n); }
  STAMP(3);
  {
    float sc[8];
#pragma unroll
    for (int g = 0; g < 8; ++g) sc[g] = P[g * 64 + pidx];
    float m = sc[0];
#pragma unroll
    for (int g = 1; g < 8; ++g) m = fmaxf(m, sc[g]);
    m = fmaxf(m, dpp<XOR1>(m)); m = fmaxf(m, dpp<XOR2>(m)); m = fmaxf(m, dpp<XOR8>(m)); m = sw16_max(m); m = sw32_max(m);
    m *= C2;
    float l = 0.f;
#pragma unroll
    for (int g = 0; g < 8; ++g) { const float p = __builtin_amdgcn_exp2f(fmaf(sc[g], C2, -m)); l += p; P[g * 64 + pidx] = p; }
    l += dpp<XOR1>(l); l += dpp<XOR2>(l); l += dpp<XOR8>(l); l = sw16_add(l, l); l = sw32_add(l, l);
    if ((lane & ~4) == 0) { ((LAS float*)(L + L_ML))[((lane >> 2) & 1) * 8 + wave] = m; ((LAS float*)(L + L_ML))[16 + ((lane >> 2) & 1) * 8 + wave] = l; }
    asm volatile("s_waitcnt lgkmcnt(0)" ::: "memory");
    unsigned arr = 0;
    if (lane == 0) arr = __hip_atomic_fetch_add((LAS unsigned*)(L + L_GM) + 5, 1u, __ATOMIC_RELAXED, __HIP_MEMORY_SCOPE_WORKGROUP);
    arr = __builtin_amdgcn_readfirstlane(arr);
    if (arr == 7) {
      asm volatile("" ::: "memory");
      const float mw = ((const LAS float*)(L + L_ML))[lane & 15], lw = ((const LAS float*)(L + L_ML))[16 + (lane & 15)];
      float M = mw;
      M = fmaxf(M, dpp<XOR1>(M)); M = fmaxf(M, dpp<XOR2>(M)); M = fmaxf(M, dpp<XOR7>(M));
      M = round24(M);
      float Ls = lw * __builtin_amdgcn_exp2f(mw - M);
      Ls += dpp<XOR1>(Ls); Ls += dpp<XOR2>(Ls); Ls += dpp<XOR7>(Ls);
      const u64 e0 = enc(M, tl24, th), e1 = enc(Ls, tl24, th);
      const v4u pv = {(unsigned)e0, (unsigned)(e0 >> 32), (unsigned)e1, (unsigned)(e1 >> 32)};
      const __amdgpu_buffer_rsrc_t rm = __builtin_amdgcn_make_buffer_rsrc((char*)a.ws + WS_G1M + (h * 32 + s) * 32, 0, 32, 0x00020000);
      if ((lane & 55) == 0) __builtin_amdgcn_raw_buffer_store_b128(pv, rm, (unsigned)(lane * 2), 0, 16);
    }
  }
  f32x2 acc0[4], acc1[4];
#pragma unroll
  for (int k = 0; k < 4; ++k) { acc0[k] = f32x2{0.f, 0.f}; acc1[k] = f32x2{0.f, 0.f}; }
  const LAS f32x4* P4 = (const LAS f32x4*)P + r;
#define VTILE(m8_, i_) do { \
    const f32x4 p0_ = P4[(i_) * 4], p1_ = P4[(i_) * 4 + 2]; \
    _Pragma("unroll") for (int t_ = 0; t_ < T; ++t_) { \
      _Pragma("unroll") for (int k_ = 0; k_ < 4; ++k_) { const f32x2 v_ = {bflo(bu[m8_][t_][k_]), bfhi(bu[m8_][t_][k_])}; \
        acc0[k_] = __builtin_elementwise_fma(f32x2{p0_[t_], p0_[t_]}, v_, acc0[k_]); \
        acc1[k_] = __builtin_elementwise_fma(f32x2{p1_[t_], p1_[t_]}, v_, acc1[k_]); } } \
    asm volatile("" : "+v"(acc0[0]), "+v"(acc0[1]), "+v"(acc0[2]), "+v"(acc0[3]), "+v"(acc1[0]), "+v"(acc1[1]), "+v"(acc1[2]), "+v"(acc1[3])); \
    __builtin_amdgcn_sched_barrier(0); } while (0)
#pragma unroll
  for (int n = LG; n < ST; ++n) { LDR(rv, n, n); if (n == LG) STAMP(24); VTILE(n - LG, n - LG); }
  const __amdgpu_buffer_rsrc_t rf = __builtin_amdgcn_make_buffer_rsrc((char*)a.ws + (wave == 7 ? WS_F + b * 1024 : WS_G1M + h * 1024), 0, ((wave == 7 && fvalid) || wave == 6) ? 1024 : 0, 0x00020000);
  v4u fgr = __builtin_amdgcn_raw_buffer_load_b128(rf, voff, 0, 16);
  bool gotml = wave != 6;
#define MLTRY() do { \
    const bool ok = (fgr[1] == th) & (fgr[3] == th) & (((fgr[0] ^ tl24) | (fgr[2] ^ tl24)) < 0x1000000u); \
    if (__all(ok) || ++mspin > kSpin) { \
      const float Mp = as_f(fgr[0] << 8), Lp = as_f(fgr[2] << 8);        \
      float Mx = Mp; \
      Mx = fmaxf(Mx, dpp<XOR2>(Mx)); Mx = fmaxf(Mx, dpp<ROR4>(Mx)); Mx = fmaxf(Mx, dpp<XOR8>(Mx)); Mx = sw16_max(Mx); Mx = sw32_max(Mx); \
      float Lt = Lp * __builtin_amdgcn_exp2f(Mp - Mx); \
      Lt += dpp<XOR2>(Lt); Lt += dpp<ROR4>(Lt); Lt += dpp<XOR8>(Lt); Lt = sw16_add(Lt, Lt); Lt = sw32_add(Lt, Lt); \
      if (lane < 2) { ((LAS float*)(L + L_GM))[lane] = Mx; ((LAS float*)(L + L_GM))[2 + lane] = 1.f / Lt; } \
      asm volatile("s_waitcnt lgkmcnt(0)" ::: "memory"); \
      if (lane == 0) ((volatile LAS unsigned*)(L + L_GM))[4] = 1u; \
      gotml = true; \
    } } while (0)
#define MLLD() do { asm volatile("" ::: "memory"); fgr = __builtin_amdgcn_raw_buffer_load_b128(rf, voff, 0, 16); } while (0)
  unsigned mspin = 0;
#define PFLD() do {} while (0)
#define PFUSE() do {} while (0)
  __builtin_amdgcn_sched_barrier(0);
#pragma unroll 1
  for (int i = ST; i < NT; i += ST) {
#pragma unroll
    for (int u = 0; u < ST; ++u) { LDR(rv, u, i + u); if (u == 0) STAMP(24 + i / 8); if (u == 7 && i == 24) STAMP(28); VTILE((u + ST - LG) % ST, i + u - LG); }
    if (!gotml) MLTRY();
    if (wave == 7 && i == ST) {
      if (fvalid) {
        for (unsigned spin = 0;; ++spin) {
          const bool ok = (fgr[1] == th) & (fgr[3] == th) & (((fgr[0] ^ tl24) | (fgr[2] ^ tl24)) < 0x1000000u);
          if (__all(ok) || spin > kSpin) break;
          __builtin_amdgcn_s_sleep(1);
          asm volatile("" ::: "memory");
          fgr = __builtin_amdgcn_raw_buffer_load_b128(rf, voff, 0, 16);
        }
      }
      const float gate = ((const LAS float*)(L + L_HV))[8];
      float hv2[2];
#pragma unroll
      for (int k = 0; k < 2; ++k) {
        float sv = as_f(fgr[2 * k] << 8);
        sv += dpp<XOR8>(sv); sv = sw16_add(sv, sv); sv = sw32_add(sv, sv);
        const float up = dpp<ROR4>(sv);
        const float y = 0.7978845608028654f * (sv + 0.044715f * sv * sv * sv);
        const float th_ = 1.f - 2.f / (__expf(2.f * y) + 1.f);
        const float hv = 0.5f * sv * (1.f + th_) * up * (1.f - gate);
        hv2[k] = fvalid ? hv : 0.f;
      }
      if (lane < 4) { ((LAS float*)(L + L_HV))[lane * 2] = hv2[0]; ((LAS float*)(L + L_HV))[lane * 2 + 1] = hv2[1]; }
    }
    PFUSE();
    MLLD(); PFLD();
    __builtin_amdgcn_sched_barrier(0);
  }
  while (!gotml) { MLTRY(); if (!gotml) { __builtin_amdgcn_s_sleep(1); MLLD(); } }
  float wt;
  {
    volatile LAS unsigned* fl = (volatile LAS unsigned*)(L + L_GM) + 4;
    while (*fl == 0u) __builtin_amdgcn_s_sleep(1);
    asm volatile("" ::: "memory");
    wt = __builtin_amdgcn_exp2f(((const LAS float*)(L + L_ML))[r * 8 + wave] - ((const LAS float*)(L + L_GM))[r]) * ((const LAS float*)(L + L_GM))[2 + r];
  }
  __builtin_amdgcn_sched_barrier(0);
#pragma unroll
  for (int n = 0; n < LG; ++n) VTILE((NT - LG + n) % ST, NT - LG + n);
  STAMP(4);
  PFUSE();
  {
    float o[8];
#pragma unroll
    for (int k = 0; k < 8; ++k) o[k] = wt * sw32_add(acc0[k >> 1][k & 1], acc1[k >> 1][k & 1]);
    LAS f32x4* mg = (LAS f32x4*)(L + L_MG) + wave * 128 + lane * 2;
    mg[0] = f32x4{o[0], o[1], o[2], o[3]}; mg[1] = f32x4{o[4], o[5], o[6], o[7]};
  }
  wg_barrier();
  STAMP(5);
  float f0 = 0.f, f1 = 0.f, gate = 0.f;
  unsigned wov[16];
  if (wave < 4) {
    {
      const LAS f32x2* mg = (const LAS f32x2*)(L + L_MG) + tid;
      f32x2 o = mg[0];
#pragma unroll
      for (int w = 1; w < 8; ++w) o += mg[w * 256];
      const u64 e0 = enc(o[0], tl24, th), e1 = enc(o[1], tl24, th);
      const v4u pv = {(unsigned)e0, (unsigned)(e0 >> 32), (unsigned)e1, (unsigned)(e1 >> 32)};
      __builtin_amdgcn_raw_buffer_store_b128(pv, r1, (unsigned)(((tid >> 3) * 32 + s) * 128 + (tid & 7) * 16), 0, 16);
    }
    STAMP(6);
    {
      unsigned wdv[8];
      const LAS unsigned* wl = (const LAS unsigned*)(L + L_W) + (tid < 224 ? tid : 0);
#pragma unroll
      for (int j = 0; j < 16; ++j) wov[j] = wl[j * 224];
#pragma unroll
      for (int j = 0; j < 8; ++j) wdv[j] = wl[(16 + j) * 224];
      const LAS f32x4* hp = (const LAS f32x4*)(L + L_HV);
      const f32x4 ha = hp[0], hb = hp[1];
      gate = ((const LAS float*)(L + L_HV))[8];
#pragma unroll
      for (int j = 0; j < 8; ++j) { const float hj = j < 4 ? ha[j & 3] : hb[j & 3]; f0 = fmaf(hj, bflo(wdv[j]), f0); f1 = fmaf(hj, bfhi(wdv[j]), f1); }
    }
    v4u g;
    __builtin_amdgcn_s_sleep(PDELAY);
    asm volatile("" ::: "memory");
    v4u ga = LD1();
    for (unsigned spin = 0;; ++spin) {
      if (__all(TAGOK(ga)) || spin > kSpin) break;
      __builtin_amdgcn_s_sleep(1);
      asm volatile("" ::: "memory"); ga = LD1();
    }
    g = ga;
    float t = sw32_add(as_f(g[0] << 8), as_f(g[2] << 8));
    t += dpp<XOR8>(t); t = sw16_add(t, t);
    if ((lane & 24) == 0) ((LAS float*)(L + L_H1))[((lane & 7) * 2 + r) * 4 + wave] = t;
  }
  STAMP(7);
  wg_barrier();
  if (wave < 4) {
    const f32x4 pa = ((const LAS f32x4*)(L + L_H1))[lane & 15];
    const float ov = (pa[0] + pa[1]) + (pa[2] + pa[3]);
    float r0 = 0.f, r1_ = 0.f;
#pragma unroll
    for (int j = 0; j < 16; ++j) { const float oj = as_f(__builtin_amdgcn_readlane(as_u(ov), j)); r0 = fmaf(oj, bflo(wov[j]), r0); r1_ = fmaf(oj, bfhi(wov[j]), r1_); }
    r0 = fmaf(gate, r0, f0); r1_ = fmaf(gate, r1_, f1);
    if (tid < 224) {
      const u64 e0 = enc(r0, tl24, th), e1 = enc(r1_, tl24, th);
      const v4u pv = {(unsigned)e0, (unsigned)(e0 >> 32), (unsigned)e1, (unsigned)(e1 >> 32)};
      __builtin_amdgcn_raw_buffer_store_b128(pv, r2, (unsigned)((((2 * tid / RC) * 256 + b) * RC + (2 * tid) % RC) * 8), 0, 16);
    }
  }
  STAMP(8);
  if (b < NRED) {
    v4u ga[RL], gb[RL], gc[RL], g[RL];
    __builtin_amdgcn_s_sleep(PDELAY2);
#define OK2(x_) ({ bool ok_ = true; unsigned y_ = 0; _Pragma("unroll") for (int k = 0; k < RL; ++k) { ok_ &= (x_[k][1] == th) & (x_[k][3] == th); y_ |= (x_[k][0] ^ tl24) | (x_[k][2] ^ tl24); } (bool)__all(ok_ & (y_ < 0x1000000u)); })
#define TAKE2(x_) do { _Pragma("unroll") for (int k = 0; k < RL; ++k) g[k] = x_[k]; } while (0)
    LD2(ga);
    for (unsigned spin = 0;; ++spin) {
      if (OK2(ga) || spin > kSpin) break;
      __builtin_amdgcn_s_sleep(1);
      LD2(ga);
    }
    TAKE2(ga);
    float u0 = as_f(g[0][0] << 8), u1 = as_f(g[0][2] << 8);
#pragma unroll
    for (int k = 1; k < RL; ++k) { u0 += as_f(g[k][0] << 8); u1 += as_f(g[k][2] << 8); }
    float u = sw32_add(u0, u1);
    u = sw16_add(u, u);
    u += dpp<XOR8>(u);
    if (RC <= 8) u += dpp<ROR4>(u);
    if (RC <= 4) u += dpp<XOR2>(u);
    if ((lane & (31 & ~(RC / 2 - 1))) == 0) ((LAS float*)(L + L_H2))[((lane & (RC / 2 - 1)) * 2 + r) * 8 + wave] = u;
    wg_barrier();
    if (tid < RC) {
      const LAS f32x4* hp = (const LAS f32x4*)(L + L_H2) + tid * 2;
      const f32x4 pa = hp[0], pb = hp[1];
      const float ov = ((pa[0] + pa[1]) + (pa[2] + pa[3])) + ((pb[0] + pb[1]) + (pb[2] + pb[3]));
      a.out[b * RC + tid] = (unsigned short)f2bf(ov);
    }
    if (b == 0 && tid == 0) __hip_atomic_store((GAS u64*)(a.ws + WS_EPOCH), ep1, RLX_AGENT);
  }
  STAMP(9);
}
}

extern "C" void kernel_launch(void* const* d_in, const int* in_sizes, int n_in, void* d_out, int out_size, void* d_ws, size_t ws_size, hipStream_t stream) {
  Args a;
  a.x = (const unsigned short*)d_in[0]; a.K = (const unsigned short*)d_in[1]; a.V = (const unsigned short*)d_in[2];
  a.wq = (const unsigned short*)d_in[3]; a.wo = (const unsigned short*)d_in[4];
  a.wg = (const unsigned short*)d_in[6]; a.wu = (const unsigned short*)d_in[7]; a.wd = (const unsigned short*)d_in[8]; a.gw = (const unsigned short*)d_in[9];
  a.out = (unsigned short*)d_out; a.ws = (unsigned char*)d_ws;
  hipLaunchKernelGGL(fused_kernel, dim3(NWG), dim3(NTHR), L_TOT, stream, a);
}
```
